```python
import torch

def _softmax(x, axis=-1):
    return torch.softmax(x, dim=axis)
def _gelu(x):
    return torch.nn.functional.gelu(x, approximate="tanh")
def _silu(x):
    return torch.nn.functional.silu(x)
def _sigmoid(x):
    return torch.sigmoid(x)
def _layernorm(x, axis=-1, eps=1e-5):
    m = x.mean(dim=axis, keepdim=True)
    v = x.var(dim=axis, keepdim=True, unbiased=False)
    return (x - m) / torch.sqrt(v + eps)
def _scatter_add(tgt, idx, src, axis=0):
    return tgt.clone().index_add_(axis, idx, src)
def _segment_sum(src, seg, n):
    out = torch.zeros((n,) + src.shape[1:], dtype=src.dtype)
    return out.index_add_(0, seg, src)
def _pad(x, pw):
    flat = tuple(v for pair in reversed(pw) for v in pair)
    return torch.nn.functional.pad(x, flat)

kernel_name = 'standalone__mha_block_cached__near_ridge'

def setup_inputs():

    _w = (lambda a: (lambda t: t.to(torch.bfloat16) if t.is_floating_point() else t)(torch.as_tensor(a)))
    B, Sq, D = 16, 96, 640
    H, Sk, Dh = 32, 512, 128
    return {
        "x": _w((lambda *s: torch.randn(*s, dtype=torch.bfloat16) * 0.8)(B, Sq, D)),
        "k_cache": _w((lambda *s: torch.randn(*s, dtype=torch.bfloat16) * 0.8)(B, H, Sk, Dh)),
        "v_cache": _w((lambda *s: torch.randn(*s, dtype=torch.bfloat16) * 0.05)(B, H, Sk, Dh)),
        "wq": _w((lambda *s: torch.randn(*s, dtype=torch.bfloat16) * 0.8)(D, H * Dh) * (D ** -0.5)),
        "wo": _w((lambda *s: torch.randn(*s, dtype=torch.bfloat16) * 0.05)(H * Dh, D) * ((H * Dh) ** -0.5)),
    }

def reference(x, k_cache, v_cache, wq, wo):
    B, Sq, D = x.shape
    H, Dh = 32, 128
    q = (lambda x, *d: x.permute(*d))((x @ wq).reshape(B, Sq, H, Dh), 0, 2, 1, 3)
    scores = torch.einsum("bhqd,bhkd->bhqk", q, k_cache) * (Dh ** -0.5)
    p = _softmax(scores, axis=-1)
    o = torch.einsum("bhqk,bhkd->bhqd", p, v_cache)
    return (lambda x, *d: x.permute(*d))(o, 0, 2, 1, 3).reshape(B, Sq, H * Dh) @ wo
```

```cpp
#include <hip/hip_runtime.h>
#include <stdint.h>
#include <stdio.h>

#define GAS __attribute__((address_space(1)))
#define LAS __attribute__((address_space(3)))
typedef __bf16 bf16x8 __attribute__((ext_vector_type(8)));
typedef __bf16 bf16x2 __attribute__((ext_vector_type(2)));
typedef short s16x4 __attribute__((ext_vector_type(4)));
typedef short s16x8 __attribute__((ext_vector_type(8)));
typedef float f32x2 __attribute__((ext_vector_type(2)));
typedef float f32x4 __attribute__((ext_vector_type(4)));
typedef float f32x16 __attribute__((ext_vector_type(16)));
typedef unsigned u32x2 __attribute__((ext_vector_type(2)));
typedef unsigned u32x4 __attribute__((ext_vector_type(4)));
#define DI __device__ __forceinline__

constexpr int NB = 16, SQ = 96, DM = 640, NH = 32, SK = 512, DH = 128;
constexpr bool KV_NT = true;
constexpr int KV_INFL = 3;
constexpr int KV_START = 7;
constexpr int TOUCH_AT = 26;
#define CODE_PF 1

constexpr size_t WS_EPOCH = 0;
constexpr size_t WS_FLW = 1024;
constexpr size_t WS_FLO = 4096;
constexpr size_t WS_FLP = 12288;
constexpr size_t WS_OT  = 16384 + 8192;
constexpr size_t WS_WOT = WS_OT + (size_t)96 * 128 * 1024;
constexpr size_t WS_PART = WS_WOT + (size_t)128 * 40 * 1024;

constexpr int SLOT = 16384, NSLOT = 5, NWSLOT = 4;
constexpr int WRING = NSLOT * SLOT;
constexpr int STG = 4 * SLOT, STG_BYTES = 28672, STG_W = 12288;
constexpr int QA_OFF = WRING, QB_OFF = WRING;
constexpr int FLAG_OFF = STG + 3 * STG_BYTES;
constexpr int WPARK = FLAG_OFF + 256;
constexpr int DUMMY = WPARK + 3072;
constexpr int LDS_A = DUMMY + 256;
constexpr int F_FULL = 0, F_FREE = 16, F_ABORT = 32, F_FULLW = 48, F_FREEW = 64, F_QRDY = 80, F_QREAD = 96, F_QRDY3 = 112, F_OARR = 128  , F_ORDY = 144  , F_WARR = 160, F_TAG = 176;

DI unsigned rfl(unsigned x) { return __builtin_amdgcn_readfirstlane(x); }
DI unsigned cvtpk(float lo, float hi) { f32x2 v = {lo, hi}; bf16x2 b = __builtin_convertvector(v, bf16x2); return __builtin_bit_cast(unsigned, b); }
DI u32x4 make_srd(const void* p, unsigned bytes) { u32x4 s = {(unsigned)(uintptr_t)p, (unsigned)((uintptr_t)p >> 32) & 0xffffu, bytes, 0x00020000u}; return s; }
template <bool NT>
DI void dma4(unsigned ldsaddr, unsigned v0, unsigned v1, unsigned v2, unsigned v3, u32x4 srd, unsigned soff) {
  if (NT)
    asm volatile("s_mov_b32 m0, %0\n\ts_nop 0\n\tbuffer_load_dwordx4 %1, %5, %6 offen nt lds\n\tbuffer_load_dwordx4 %2, %5, %6 offen offset:1024 nt lds\n\t"
                 "buffer_load_dwordx4 %3, %5, %6 offen offset:2048 nt lds\n\tbuffer_load_dwordx4 %4, %5, %6 offen offset:3072 nt lds"
                 :: "s"(ldsaddr), "v"(v0), "v"(v1), "v"(v2), "v"(v3), "s"(srd), "s"(soff) : "m0", "memory");
  else
    asm volatile("s_mov_b32 m0, %0\n\ts_nop 0\n\tbuffer_load_dwordx4 %1, %5, %6 offen lds\n\tbuffer_load_dwordx4 %2, %5, %6 offen offset:1024 lds\n\t"
                 "buffer_load_dwordx4 %3, %5, %6 offen offset:2048 lds\n\tbuffer_load_dwordx4 %4, %5, %6 offen offset:3072 lds"
                 :: "s"(ldsaddr), "v"(v0), "v"(v1), "v"(v2), "v"(v3), "s"(srd), "s"(soff) : "m0", "memory");
}
DI void dma4s(unsigned ldsaddr, unsigned v0, unsigned v1, unsigned v2, unsigned v3, u32x4 srd, unsigned soff) {
  asm volatile("s_mov_b32 m0, %0\n\ts_nop 0\n\tbuffer_load_dwordx4 %1, %5, %6 offen sc1 lds\n\tbuffer_load_dwordx4 %2, %5, %6 offen offset:1024 sc1 lds\n\t"
               "buffer_load_dwordx4 %3, %5, %6 offen offset:2048 sc1 lds\n\tbuffer_load_dwordx4 %4, %5, %6 offen offset:3072 sc1 lds"
               :: "s"(ldsaddr), "v"(v0), "v"(v1), "v"(v2), "v"(v3), "s"(srd), "s"(soff) : "m0", "memory");
}
DI void dma2s(unsigned ldsaddr, unsigned v0, unsigned v1, u32x4 srd, unsigned soff) {
  asm volatile("s_mov_b32 m0, %0\n\ts_nop 0\n\tbuffer_load_dwordx4 %1, %3, %4 offen sc1 lds\n\tbuffer_load_dwordx4 %2, %3, %4 offen offset:1024 sc1 lds"
               :: "s"(ldsaddr), "v"(v0), "v"(v1), "s"(srd), "s"(soff) : "m0", "memory");
}
DI void touch64(unsigned ldsaddr, unsigned voff, u32x4 srd, unsigned soff) {
  asm volatile("s_mov_b32 m0, %0\n\ts_nop 0\n\tbuffer_load_dword %1, %2, %3 offen sc1 lds" :: "s"(ldsaddr), "v"(voff), "s"(srd), "s"(soff) : "m0", "memory");
}
template <bool NT>
DI void dma_tile(unsigned dst, const unsigned (&vo)[4], u32x4 srd, unsigned soff, unsigned gstep) {
#pragma unroll
  for (int g4 = 0; g4 < 4; ++g4) dma4<NT>(dst + 4096 * g4, vo[0], vo[1], vo[2], vo[3], srd, soff + gstep * g4);
}
#define WAIT_VM(n) asm volatile("s_waitcnt vmcnt(" #n ")" ::: "memory")
#define WAIT_LGKM0() asm volatile("s_waitcnt lgkmcnt(0)" ::: "memory")
#define CBAR() asm volatile("" ::: "memory")
#define SB() __builtin_amdgcn_sched_barrier(0)
#define PIN(x) asm volatile("" : "+v"(x))
#define PIN2(x, y) asm volatile("" : "+v"(x), "+v"(y))
DI void bar() { asm volatile("s_waitcnt lgkmcnt(0)\n\ts_barrier" ::: "memory"); }
DI bf16x8 ld_b128(LAS char* p) { return *(LAS bf16x8*)p; }
DI bf16x8 ld_tr2(LAS char* p0, LAS char* p1) {
  s16x4 a = __builtin_amdgcn_ds_read_tr16_b64_v4i16((LAS s16x4*)p0), b = __builtin_amdgcn_ds_read_tr16_b64_v4i16((LAS s16x4*)p1);
  s16x8 ab = __builtin_shufflevector(a, b, 0, 1, 2, 3, 4, 5, 6, 7); return __builtin_bit_cast(bf16x8, ab);
}
DI void flag_set(unsigned addr_v, unsigned val) {
  unsigned tmp;
  asm volatile("v_mov_b32 %0, %2\n\ts_mov_b64 exec, 1\n\tds_write_b32 %1, %0\n\ts_mov_b64 exec, -1" : "=&v"(tmp) : "v"(addr_v), "s"(val) : "memory");
}
DI unsigned flag_get(LAS char* L, int off) { return *(volatile LAS unsigned*)(L + off); }
DI unsigned flag_min3(LAS char* L, int off) { const u32x4 f = *(volatile LAS u32x4*)(L + off); const unsigned a = f.x < f.y ? f.x : f.y; return a < f.z ? a : f.z; }
template <bool MIN3, bool SLEEP>
DI void spin_slow(LAS char* L, int off, unsigned need) {
  unsigned it = 0;
  while (rfl(MIN3 ? flag_min3(L, off) : flag_get(L, off)) < need) {
    if (SLEEP) __builtin_amdgcn_s_sleep(1);
    if ((++it & 1023u) == 0u) { if (rfl(flag_get(L, FLAG_OFF + F_ABORT)) != 0u) break; if (it > (1u << 21)) { *(volatile LAS unsigned*)(L + FLAG_OFF + F_ABORT) = 1u; break; } }
  }
}
template <bool MIN3, bool SLEEP>
DI void wait_ge(LAS char* L, int off, unsigned need, unsigned prefetched) {
  if (__builtin_expect(rfl(prefetched) < need, 0)) spin_slow<MIN3, SLEEP>(L, off, need);
  CBAR();
}
#define MFMA32(a, b, c) __builtin_amdgcn_mfma_f32_32x32x16_bf16((a), (b), (c), 0, 0, 0)
#define MFMA16(a, b, c) __builtin_amdgcn_mfma_f32_16x16x32_bf16((a), (b), (c), 0, 0, 0)
constexpr float CS = 0.08838834764831845f * 1.4426950408889634f;
constexpr float THR = 8.0f;
DI void mfma16_acc(f32x4& acc, const bf16x8& a, const bf16x8& b) { asm("v_mfma_f32_16x16x32_bf16 %0, %1, %2, %0" : "+v"(acc) : "v"(a), "v"(b)); }
DI float max3f(float a, float b, float c) { float r; asm("v_max3_f32 %0, %1, %2, %3" : "=v"(r) : "v"(a), "v"(b), "v"(c)); return r; }

DI unsigned vm_outstanding() { const unsigned ib = __builtin_amdgcn_s_getreg((31 << 11) | 7); return (ib & 15u) | ((ib >> 18) & 0x30u); }
template <int NS, int TOTAL, class Issue, class MaxFl, class Gate>
DI void ring_loader(LAS char* L, unsigned fullv, int free_off, int S0, Issue issue, MaxFl maxfl, Gate gate) {
  int S = S0, P = 0; unsigned it = 0;
  for (;;) {
    const int landed = S - (int)((vm_outstanding() + 15u) >> 4);
    if (landed > P) {
      asm volatile("" ::: "memory");
      P = landed; flag_set(fullv, P);
      if (P == TOTAL) break;
    }
    bool did = false;
    if (S < TOTAL && S - landed < maxfl(S)) {
      bool ok = S < NS || rfl(flag_min3(L, free_off)) >= (unsigned)(S - NS + 1);
      if (ok) ok = gate(S);
      if (ok) { CBAR(); issue(S); ++S; did = true; }
    }
    if (!did) {
      __builtin_amdgcn_s_sleep(1);
      if ((++it & 4095u) == 0u) { if (rfl(flag_get(L, FLAG_OFF + F_ABORT)) != 0u) break; if (it > (1u << 22)) { *(volatile LAS unsigned*)(L + FLAG_OFF + F_ABORT) = 1u; break; } }
    }
  }
  asm volatile("s_waitcnt vmcnt(0)" ::: "memory");
}
DI unsigned fresh_lane() { unsigned l; asm volatile("v_mbcnt_lo_u32_b32 %0, -1, 0\n\tv_mbcnt_hi_u32_b32 %0, -1, %0" : "=v"(l)); return l; }
typedef GAS unsigned gu32;
#define RLX_AGENT __ATOMIC_RELAXED, __HIP_MEMORY_SCOPE_AGENT
constexpr int AUX_SC1 = 16;
template <int NW>
DI void poll_tag(const char* p, unsigned tag, LAS char* L) {
  const unsigned fl = fresh_lane();
  unsigned it = 0;
  for (;;) {
    bool ok;
    if (NW == 128) { const unsigned long long v = __hip_atomic_load((GAS unsigned long long*)(p + fl * 8), RLX_AGENT); ok = (unsigned)v == tag && (unsigned)(v >> 32) == tag; }
    else { const unsigned v = __hip_atomic_load((gu32*)(p + (fl & (NW - 1)) * 4), RLX_AGENT); ok = v == tag; }
    if (__all(ok)) break;
    __builtin_amdgcn_s_sleep(2);
    if ((++it & 255u) == 0u) { if (rfl(flag_get(L, FLAG_OFF + F_ABORT)) != 0u) break; if (it > (1u << 17)) { *(volatile LAS unsigned*)(L + FLAG_OFF + F_ABORT) = 1u; break; } }
  }
  __builtin_amdgcn_fence(__ATOMIC_ACQUIRE, "wavefront");
}
template <bool DO_QK>
DI void blockB(f32x16& n0, f32x16& n1, f32x16& c0, f32x16& c1, const f32x16& cin, const bf16x8 (&qf)[8], bf16x8 (&pf)[4], LAS char* kt, unsigned ka0, unsigned ka1) {
  bf16x8 kf[4];
  u32x4 pw[4];
  auto kaddr = [&](int i) { const int s = i >> 1, kb = i & 1; return kt + ((s & 1) ? ka1 : ka0) + 512 * (s >> 1) + 8192 * kb; };
  if (DO_QK) { kf[0] = ld_b128(kaddr(0)); kf[1] = ld_b128(kaddr(1)); kf[2] = ld_b128(kaddr(2)); }
  { float t0 = __builtin_amdgcn_exp2f(c0[0]), t1 = __builtin_amdgcn_exp2f(c0[1]); PIN2(t0, t1); c0[0] = t0; c0[1] = t1; }
  { float t0 = __builtin_amdgcn_exp2f(c0[2]), t1 = __builtin_amdgcn_exp2f(c0[3]); PIN2(t0, t1); c0[2] = t0; c0[3] = t1; }
  SB();
#pragma unroll
  for (int i = 0; i < 16; ++i) {
    if (DO_QK) {
      if ((i & 1) == 0) { if (i + 2 < 16) __builtin_amdgcn_s_waitcnt(0xC07F | (1 << 8)); else __builtin_amdgcn_s_waitcnt(0xC07F); SB(); }
      if (i + 3 < 16) kf[(i + 3) & 3] = ld_b128(kaddr(i + 3));
      const int s = i >> 1;
      if ((i & 1) == 0) n0 = MFMA32(kf[i & 3], qf[s], s == 0 ? cin : n0); else n1 = MFMA32(kf[i & 3], qf[s], s == 0 ? cin : n1);
    }
    if (i + 2 < 16) { const int ii = i + 2; f32x16& c = ii < 8 ? c0 : c1; const int e = (2 * ii) & 15;
      float t0 = __builtin_amdgcn_exp2f(c[e]), t1 = __builtin_amdgcn_exp2f(c[e + 1]); PIN2(t0, t1); c[e] = t0; c[e + 1] = t1; }
    { f32x16& cp = i < 8 ? c0 : c1; const int ep = (2 * i) & 15; unsigned w = cvtpk(cp[ep], cp[ep + 1]); PIN(w); pw[i >> 2][i & 3] = w; }
    SB();
  }
#pragma unroll
  for (int s = 0; s < 4; ++s) pf[s] = __builtin_bit_cast(bf16x8, pw[s]);
}
template <bool DO_MAX>
DI void blockC(f32x16 (&o)[4], const bf16x8 (&pf)[4], const f32x16& c0, const f32x16& c1, const f32x16& n0, const f32x16& n1, float& l0, float& l1, float& mx,
               LAS char* vt, unsigned va0, unsigned va1, LAS char* L, unsigned& fv) {
  bf16x8 vf[4];
  auto vld = [&](int j) { const int s = j >> 2, d = j & 3; return ld_tr2(vt + va0 + 4096 * s + 512 * d, vt + va1 + 4096 * s + 512 * d); };
  vf[0] = vld(0); vf[1] = vld(1); vf[2] = vld(2);
  { l0 += c0[0]; l1 += c0[1]; PIN2(l0, l1); l0 += c0[2]; l1 += c0[3]; PIN2(l0, l1); }
  SB();
#pragma unroll
  for (int j = 0; j < 16; ++j) {
    if ((j & 1) == 0) { if (j + 2 < 16) __builtin_amdgcn_s_waitcnt(0xC07F | (2 << 8)); else __builtin_amdgcn_s_waitcnt(0xC07F); SB(); }
    if (j + 3 < 16) vf[(j + 3) & 3] = vld(j + 3);
    o[j & 3] = MFMA32(vf[j & 3], pf[j >> 2], o[j & 3]);
    if (j + 2 < 16) { const int jj = j + 2; const f32x16& c = jj < 8 ? c0 : c1; const int e = (2 * jj) & 15; l0 += c[e]; l1 += c[e + 1]; PIN2(l0, l1); }
    if (DO_MAX) { const f32x16& n = j < 8 ? n0 : n1; const int e = (2 * j) & 15; mx = __builtin_fmaxf(__builtin_fmaxf(j == 0 ? n[0] : mx, n[e]), n[e + 1]); PIN(mx); }
    if (j == 13) fv = flag_get(L, FLAG_OFF + F_FULL);
    SB();
  }
}
DI float xhalf_max(float x) { auto rr = __builtin_amdgcn_permlane32_swap(__float_as_uint(x), __float_as_uint(x), false, false); return fmaxf(__uint_as_float(rr[0]), __uint_as_float(rr[1])); }
DI float xhalf_sum(float x) { auto rr = __builtin_amdgcn_permlane32_swap(__float_as_uint(x), __float_as_uint(x), false, false); return __uint_as_float(rr[0]) + __uint_as_float(rr[1]); }

__global__ __launch_bounds__(512, 2) void attn_kernel(const char* __restrict__ X, const char* __restrict__ Kc, const char* __restrict__ Vc,
                                                      const char* __restrict__ Wq, const unsigned short* __restrict__ Wo, char* __restrict__ ws, unsigned short* __restrict__ out) {
  extern __shared__ __attribute__((aligned(16))) char lds_raw[];
  LAS char* L = (LAS char*)lds_raw;
  const unsigned Lbase = (unsigned)(uintptr_t)L;
  const int tid = threadIdx.x, lane = tid & 63, wave = rfl(tid >> 6);
  const int xcd = blockIdx.x & 7, cc = blockIdx.x >> 3;
  const int b = 8 * (xcd >> 2) + (cc >> 2), hp = 4 * (xcd & 3) + (cc & 3);
  const int rot = ((5 * (b & 7)) / 4 + (5 * (hp & 3)) / 2) % 10;
  unsigned cpf = 0;
  {
    unsigned long long pc; unsigned sz;
    asm volatile("s_getpc_b64 %0\n.Lmha_code_begin:\n\ts_mov_b32 %1, .Lmha_code_end-.Lmha_code_begin" : "=s"(pc), "=s"(sz));
    const unsigned off = (unsigned)(cc * 64 + lane) * 128u;
    if (wave == 3 && cc < 8 && off < sz) cpf = *(const volatile unsigned*)(pc + off);
  }
  if (tid < 44) ((LAS unsigned*)(L + FLAG_OFF))[tid] = 0u;
  const unsigned flagv = Lbase + FLAG_OFF;
  auto kslot = [&](int s) { const int k = s + rot; return k >= 10 ? k - 10 : k; };
  const int r32 = lane & 31, h = lane >> 5, blk = (lane >> 4) & 1, q4 = (lane & 15) >> 2, p4 = lane & 3;

  const int c = wave < 3 ? wave : wave - 1, qi = c >= 3 ? c - 3 : c, dh = c >= 3 ? 1 : 0;
  const u32x4 srdX = make_srd(X - 4096, (unsigned)NB * SQ * DM * 2 + 4096);
  const unsigned xa = (32 * qi + r32) * 128, xs = (r32 >> 1) & 7;
  unsigned xad[4], wa[2];
#pragma unroll
  for (int ks = 0; ks < 4; ++ks) xad[ks] = STG + xa + 16 * ((2 * ks + h) ^ xs);
#pragma unroll
  for (int t = 0; t < 2; ++t) wa[t] = STG + STG_W + 2048 * h + 64 * (4 * t + q4) + 16 * ((2 * blk + (p4 & 1)) ^ ((2 * h + t) & 3)) + 8 * (p4 >> 1);
  auto pack_frag = [&](const f32x16& a, int o) {
    u32x4 w = {cvtpk(a[o] * CS, a[o + 1] * CS), cvtpk(a[o + 2] * CS, a[o + 3] * CS), cvtpk(a[o + 4] * CS, a[o + 5] * CS), cvtpk(a[o + 6] * CS, a[o + 7] * CS)};
    return w;
  };

  if (wave < 3) {
    bf16x8 qf[8];
    {
      unsigned xv[4];
#pragma unroll
      for (int e = 0; e < 4; ++e) xv[e] = 4096u + (32 * wave + 8 * e + (lane >> 3)) * 1280 + 16 * ((lane & 7) ^ ((4 * (e & 1) + (lane >> 4)) & 7)) - 1024u * e;
      auto x_issue = [&](int s, int sbuf) { dma4<false>(Lbase + STG + sbuf * STG_BYTES + 4096 * wave, xv[0], xv[1], xv[2], xv[3], srdX, (unsigned)(b * SQ * 1280 + 128 * kslot(s))); };
      f32x16 qacc[3];
#pragma unroll
      for (int d = 0; d < 3; ++d) qacc[d] = f32x16{};
      bf16x8 xf[2][4], wf[2][4][3];
      x_issue(0, 0); x_issue(1, 1);
      int sb = 0;
      auto stp = [&](int p, int s, bool mf, bool st2) {
        LAS char* st = L + sb * STG_BYTES;
        const int sn = sb == 0 ? 2 : sb - 1;
        sb = sb == 2 ? 0 : sb + 1;
#pragma unroll
        for (int g = 0; g < 12; ++g) {
          const int ks = g / 3, d = g % 3;
          if (mf) qacc[d] = MFMA32(wf[p ^ 1][ks][d], xf[p ^ 1][ks], qacc[d]);
          if (g < 6) {
#pragma unroll
            for (int e = 2 * g; e < 2 * g + 2; ++e) wf[p][e / 3][e % 3] = ld_tr2(st + wa[0] + 4096 * (e / 3) + 512 * (e % 3), st + wa[1] + 4096 * (e / 3) + 512 * (e % 3));
          } else if (g < 8) { xf[p][2 * (g - 6)] = ld_b128(st + xad[2 * (g - 6)]); xf[p][2 * (g - 6) + 1] = ld_b128(st + xad[2 * (g - 6) + 1]); }
          if (g == 8 && st2) x_issue(s + 2, sn);
          SB();
        }
      };
#pragma unroll
      for (int ks = 0; ks < 4; ++ks) {
        xf[1][ks] = bf16x8{};
#pragma unroll
        for (int d = 0; d < 3; ++d) wf[1][ks][d] = bf16x8{};
      }
#pragma nounroll
      for (int s = 0; s < 10; s += 2) {
        WAIT_VM(4); bar();
        stp(0, s, true, s + 2 < 10);
        if (s < 8) WAIT_VM(4); else WAIT_VM(0);
        bar();
        stp(1, s + 1, true, s + 3 < 10);
      }
      bar();
#pragma unroll
      for (int g = 0; g < 12; ++g) qacc[g % 3] = MFMA32(wf[1][g / 3][g % 3], xf[1][g / 3], qacc[g % 3]);
      LAS char* Qb = L + QA_OFF + wave * 8192 + lane * 16;
#pragma unroll
      for (int f = 0; f < 6; ++f) *(LAS u32x4*)(Qb + f * 1024) = pack_frag(qacc[f >> 1], 8 * (f & 1));
    }
    const unsigned lt = fresh_lane();
    const unsigned ka0 = 2048 * ((lt & 31) >> 3) + 64 * (lt & 7) + 16 * ((lt >> 5) ^ ((lt >> 2) & 3)), ka1 = ka0 ^ 32u;
    const unsigned va0 = 64 * (4 * (lt >> 5) + ((lt & 15) >> 2)) + 16 * ((2 * ((lt >> 4) & 1) + ((lt & 3) >> 1)) ^ (lt >> 5)) + 8 * (lt & 1), va1 = (va0 ^ 32u) + 2048;
    const unsigned freev = flagv + F_FREE + 4 * wave;
    int slot = 0;
    auto next_slot = [&]() { LAS char* p = L + slot * SLOT; slot = slot + 1 == NSLOT ? 0 : slot + 1; return p; };
    f32x16 o[4], negm, sA0, sA1, sB0, sB1;
    bf16x8 pf[4];
#pragma unroll
    for (int d = 0; d < 4; ++d) o[d] = f32x16{};
    negm = f32x16{}; sB0 = f32x16{} - 1e30f; sB1 = sB0;
    float l0 = 0.f, l1 = 0.f, mx;
    unsigned fv = 0;
    const __amdgpu_buffer_rsrc_t rsO = __builtin_amdgcn_make_buffer_rsrc((void*)(ws + WS_OT), 0, 96 * 128 * 1024, 0x00020000);
    auto o_publish = [&](int u) {
      WAIT_VM(0);
      unsigned old = 0;
      const unsigned fl = fresh_lane();
      if (fl == 0) old = __hip_atomic_fetch_add((LAS unsigned*)(L + FLAG_OFF + F_OARR + 4 * u), 1u, __ATOMIC_RELAXED, __HIP_MEMORY_SCOPE_WORKGROUP);
      if (rfl(old) == 2u) { const unsigned tag = rfl(flag_get(L, FLAG_OFF + F_TAG)); if (fl == 0) __hip_atomic_store((gu32*)(ws + WS_FLO + (size_t)(4 * b + (hp >> 2)) * 128 + (4 * u + (hp & 3)) * 4), tag, RLX_AGENT); }
    };
    auto step = [&](f32x16& c0, f32x16& c1, f32x16& n0, f32x16& n1, int sg, bool EVEN) {
      const bool bnd = EVEN && (sg & 7) == 0, first = EVEN && sg == 0, last = EVEN && sg == 16;
      const unsigned needK = first ? 1u : 2u * sg, needV = last ? 32u : 2u * sg + 1u;
      if (!last) wait_ge<false, false>(L, FLAG_OFF + F_FULL, first ? needK : needV, fv);
      LAS char* kt = L + slot * SLOT;
      if (!last) slot = slot + 1 == NSLOT ? 0 : slot + 1;
      blockB<true>(n0, n1, c0, c1, negm, qf, pf, kt, ka0, ka1);
      if (last) wait_ge<false, false>(L, FLAG_OFF + F_FULL, needV, fv); else if (!first) flag_set(freev, needK);
      LAS char* vt = first ? kt : L + slot * SLOT;
      if (!first) slot = slot + 1 == NSLOT ? 0 : slot + 1;
      blockC<true>(o, pf, c0, c1, n0, n1, l0, l1, mx, vt, va0, va1, L, fv);
      flag_set(freev, first ? needK : needV);
      if (!EVEN && sg == 9) o_publish(0);
      mx = xhalf_max(mx);
      if (bnd || __builtin_expect(__any(mx > THR), 0)) {
        if (bnd && !first) {
          const int u = (sg >> 3) - 1;
          const float inv = 1.0f / xhalf_sum(l0 + l1);
          const unsigned fl = fresh_lane();
          const unsigned ov = ((fl >> 4) & 1) * 131072 + 16 * ((fl & 15) + 16 * (fl >> 5)), os = (unsigned)(((6 * b + 2 * wave) * 128 + 4 * (hp + 16 * u)) * 1024);
#pragma unroll
          for (int d = 0; d < 4; ++d)
#pragma unroll
            for (int uu = 0; uu < 2; ++uu) {
              u32x4 w = {cvtpk(o[d][8 * uu] * inv, o[d][8 * uu + 1] * inv), cvtpk(o[d][8 * uu + 2] * inv, o[d][8 * uu + 3] * inv),
                         cvtpk(o[d][8 * uu + 4] * inv, o[d][8 * uu + 5] * inv), cvtpk(o[d][8 * uu + 6] * inv, o[d][8 * uu + 7] * inv)};
              __builtin_amdgcn_raw_buffer_store_b128(w, rsO, ov + os + d * 1024 + 512 * uu, 0, AUX_SC1);
            }
        }
        const float dl = bnd ? mx : fmaxf(mx, 0.f), al = bnd ? 0.f : __builtin_amdgcn_exp2f(-dl);
        l0 *= al; l1 *= al;
#pragma unroll
        for (int r = 0; r < 16; ++r) { n0[r] -= dl; n1[r] -= dl; negm[r] -= dl; }
#pragma unroll
        for (int d = 0; d < 4; ++d)
#pragma unroll
          for (int r = 0; r < 16; ++r) o[d][r] *= al;
      }
    };
#pragma nounroll
    for (int u = 0; u < 3; ++u) {
      if (u < 2) {
        if (u == 0) spin_slow<false, false>(L, FLAG_OFF + F_QRDY3, 1);
        else spin_slow<false, false>(L, FLAG_OFF + F_QRDY + 4 * wave, 2);
        CBAR();
        LAS char* Qb = L + QA_OFF + wave * 8192 + fresh_lane() * 16;
#pragma unroll
        for (int s = 0; s < 8; ++s) qf[s] = ld_b128(Qb + 1024 * s);
        CBAR();
        flag_set(flagv + F_QREAD + 4 * wave, u + 1);
      }
#pragma nounroll
      for (int pr = 0; pr < 4; ++pr) {
        step(sB0, sB1, sA0, sA1, 8 * u + 2 * pr, true);
        if (u == 2) break;
        step(sA0, sA1, sB0, sB1, 8 * u + 2 * pr + 1, false);
      }
    }
    o_publish(1);
  } else if (wave == 3) {
    const unsigned ln3 = fresh_lane();
    const unsigned pc_row = (ln3 >> 2) & 7, pc_col = 64 * (ln3 >> 5) + 16 * ((ln3 & 3) ^ ((ln3 >> 4) & 1));
    unsigned vo[4];
#pragma unroll
    for (int m = 0; m < 4; ++m) vo[m] = 4096u + 2048u * (m >> 1) + 128u * (m & 1) + ((pc_row * 256 + pc_col) ^ (((m >> 1) & 1) ? 32u : 0u)) - 1024u * (m & 3);
    const u32x4 srdK = make_srd(Kc - 4096, (unsigned)NB * NH * SK * DH * 2 + 4096), srdV = make_srd(Vc - 4096, (unsigned)NB * NH * SK * DH * 2 + 4096);
    const int rot2 = (cc + xcd) & 7;
    const unsigned ubase = (unsigned)((b * NH + hp) * SK * 256);
    int kvslot = 0;
    auto kv_issue = [&](int n) {
      const bool isV = n == 31 || (n >= 2 && (n & 1) == 0);
      const int tau = n == 31 ? 15 : (isV ? (n >> 1) - 1 : (n + 1) >> 1);
      const unsigned base = ubase + (unsigned)((tau >> 3) * 16 * SK * 256) + (unsigned)(((tau + rot2) & 7) * 16384);
      dma_tile<KV_NT>(Lbase + kvslot * SLOT, vo, isV ? srdV : srdK, base, 4096u);
      kvslot = kvslot + 1 == NSLOT ? 0 : kvslot + 1;
    };
    int kvg = 0;
    auto kv_group = [&]() {
      const int n = kvg >> 2, g4 = kvg & 3;
      const bool isV = n >= 2 && (n & 1) == 0;
      const int tau = isV ? (n >> 1) - 1 : (n + 1) >> 1;
      const unsigned base = ubase + (unsigned)(((tau + rot2) & 7) * 16384);
      dma4<KV_NT>(Lbase + n * SLOT + 4096 * g4, vo[0], vo[1], vo[2], vo[3], isV ? srdV : srdK, base + 4096u * g4);
      ++kvg;
      if ((kvg & 3) == 0) kvslot = kvg >> 2;
    };
    {
      f32x16 qacc[3];
#pragma unroll
      for (int i = 0; i < 3; ++i) qacc[i] = f32x16{};
      unsigned xa3[4], wa3[2];
#pragma unroll
      for (int ks = 0; ks < 4; ++ks) xa3[ks] = STG + (ln3 & 31) * 128 + 16 * ((2 * ks + (ln3 >> 5)) ^ ((ln3 >> 1) & 7));
#pragma unroll
      for (int t = 0; t < 2; ++t) wa3[t] = STG + STG_W + 2048 * (ln3 >> 5) + 512 * 3 + 64 * (4 * t + ((ln3 & 15) >> 2)) + 16 * ((2 * ((ln3 >> 4) & 1) + (ln3 & 1)) ^ ((2 * (ln3 >> 5) + t) & 3)) + 8 * ((ln3 & 3) >> 1);
      int sb = 0;
      bf16x8 wf[2][4], xf[2][4][3];
      auto stp = [&](int p, bool mf) {
        LAS char* st = L + sb * STG_BYTES; sb = sb == 2 ? 0 : sb + 1;
#pragma unroll
        for (int g = 0; g < 12; ++g) {
          const int ks = g / 3, i = g % 3;
          if (mf) qacc[i] = MFMA32(wf[p ^ 1][ks], xf[p ^ 1][ks][i], qacc[i]);
          if (g < 4) { wf[p][g] = ld_tr2(st + wa3[0] + 4096 * g, st + wa3[1] + 4096 * g); xf[p][g][0] = ld_b128(st + xa3[g]); }
          else if (g < 8) { xf[p][g - 4][1] = ld_b128(st + xa3[g - 4] + 4096); xf[p][g - 4][2] = ld_b128(st + xa3[g - 4] + 8192); }
          SB();
        }
      };
#pragma unroll
      for (int ks = 0; ks < 4; ++ks) {
        wf[1][ks] = bf16x8{};
#pragma unroll
        for (int i = 0; i < 3; ++i) xf[1][ks][i] = bf16x8{};
      }
#pragma nounroll
      for (int s = 0; s < 10; s += 2) {
        bar(); stp(0, true);
        if (s >= KV_START) kv_group();
        bar(); stp(1, true);
        if (s + 1 >= KV_START) kv_group();
      }
      bar();
#pragma unroll
      for (int g = 0; g < 12; ++g) qacc[g % 3] = MFMA32(wf[1][g / 3], xf[1][g / 3][g % 3], qacc[g % 3]);
#pragma unroll
      for (int i = 0; i < 3; ++i)
#pragma unroll
        for (int f = 0; f < 2; ++f) *(LAS u32x4*)(L + QA_OFF + i * 8192 + (6 + f) * 1024 + ln3 * 16) = pack_frag(qacc[i], 8 * f);
      CBAR();
      flag_set(flagv + F_QRDY3, 1);
      while (kvg & 3) kv_group();
    }
    ring_loader<NSLOT, 32>(L, flagv + F_FULL, FLAG_OFF + F_FREE, kvg >> 2, kv_issue, [&](int S) { return KV_INFL; }, [&](int) { return true; });
  } else if (wave < 7) {
    const int gi = wave - 4;
    bf16x8 xk[10][4];
    {
      const int tkb = blockIdx.x >> 1, tnh = blockIdx.x & 1;
      const unsigned lg = fresh_lane();
      u32x4 wraw[7];
#pragma unroll
      for (int i = 0; i < 7; ++i) {
        const int f = gi + 3 * i < 20 ? gi + 3 * i : 19;
        wraw[i] = *(const u32x4*)((const char*)Wo + (size_t)(32 * tkb + (lg >> 1)) * 1280 + 32 * (20 * tnh + f) + 16 * (lg & 1));
      }
      SB();
#pragma unroll
      for (int s = 0; s < 10; ++s) {
        bar();
#pragma unroll
        for (int ks = 0; ks < 4; ++ks) xk[s][ks] = ld_b128(L + (s % 3) * STG_BYTES + xad[ks]);
        if (s >= 1 && s <= 7) {
          const int i = s - 1;
          const __amdgpu_buffer_rsrc_t rsWt = __builtin_amdgcn_make_buffer_rsrc((void*)(ws + WS_WOT), 0, 128 * 40 * 1024, 0x00020000);
          LAS char* sc = L + WPARK + gi * 1024;
          const unsigned ra = (16 * (lg >> 5) + 4 * ((lg >> 4) & 1) + ((lg & 15) >> 2)) * 32 + 8 * (lg & 3);
          *(LAS u32x4*)(sc + lg * 16) = wraw[i];
          const u32x4 w = __builtin_bit_cast(u32x4, ld_tr2(sc + ra, sc + ra + 8 * 32));
          if (gi + 3 * i < 20) __builtin_amdgcn_raw_buffer_store_b128(w, rsWt, (unsigned)((tkb * 40 + 20 * tnh + gi + 3 * i) * 1024) + lg * 16, 0, AUX_SC1);
        }
        if (s == 9) {
          WAIT_VM(0);
          unsigned old = 0;
          if (lg == 0) old = __hip_atomic_fetch_add((LAS unsigned*)(L + FLAG_OFF + F_WARR), 1u, __ATOMIC_RELAXED, __HIP_MEMORY_SCOPE_WORKGROUP);
          if (rfl(old) == 2u) { const unsigned tag = rfl(flag_get(L, FLAG_OFF + F_TAG)); if (lg == 0) __hip_atomic_store((gu32*)(ws + WS_FLW + (size_t)(tnh * 128 + tkb) * 4), tag, RLX_AGENT); }
        }
        SB();
      }
      bar();
    }
    const unsigned freev = flagv + F_FREEW + 4 * gi;
    unsigned wr[2];
#pragma unroll
    for (int t = 0; t < 2; ++t) { const unsigned lg = fresh_lane(); wr[t] = Lbase + WRING + 1024 * (lg >> 5) + 64 * (4 * t + ((lg & 15) >> 2)) + 16 * ((2 * ((lg >> 4) & 1) + (lg & 1)) ^ ((2 * (lg >> 5) + t) & 3)) + 8 * ((lg & 3) >> 1); }
    unsigned wb[2];
    int wslot = 2;
    auto wset = [&]() { wb[0] = wr[0] + wslot * SLOT; wb[1] = wr[1] + wslot * SLOT; PIN2(wb[0], wb[1]); };
    auto wld = [&](int e) { const int o = 2048 * (e >> 1) + 512 * (e & 1); return ld_tr2((LAS char*)(uintptr_t)wb[0] + o, (LAS char*)(uintptr_t)wb[1] + o); };
    u32x4 qp[4];
    f32x16 acc[2];
    unsigned fw = 0;
#pragma nounroll
    for (int pass = 0; pass < 2; ++pass) {
      acc[0] = f32x16{}; acc[1] = f32x16{};
#pragma unroll
      for (int i = 0; i < 5; ++i) {
        const int n = 5 * pass + i;
        wait_ge<false, true>(L, FLAG_OFF + F_FULLW, n + 1, fw);
        wset();
        bf16x8 wf[4];
        wf[0] = wld(0); wf[1] = wld(1); wf[2] = wld(2);
        SB();
#pragma unroll
        for (int e = 0; e < 16; ++e) {
          if (e + 3 < 16) wf[(e + 3) & 3] = wld(e + 3);
          acc[e & 1] = MFMA32(wf[e & 3], xk[2 * i + (e >> 3)][(e >> 1) & 3], acc[e & 1]);
          if (e == 13) { flag_set(freev, n + 1); fw = flag_get(L, FLAG_OFF + F_FULLW); }
          SB();
        }
        wslot = wslot + 1 == NWSLOT ? 0 : wslot + 1;
      }
      if (pass == 0) {
#pragma unroll
        for (int f = 0; f < 4; ++f) { qp[f] = pack_frag(acc[f >> 1], 8 * (f & 1)); PIN(qp[f]); }
      }
    }
    spin_slow<true, true>(L, FLAG_OFF + F_FREEW, 10); CBAR();
    {
      LAS char* Qb = L + QB_OFF + gi * 8192 + fresh_lane() * 16;
#pragma unroll
      for (int f = 0; f < 4; ++f) { *(LAS u32x4*)(Qb + f * 1024) = qp[f]; *(LAS u32x4*)(Qb + (4 + f) * 1024) = pack_frag(acc[f >> 1], 8 * (f & 1)); }
      CBAR();
      flag_set(flagv + F_QRDY + 4 * gi, 2);
    }
  } else {
    const u32x4 srdW = make_srd(Wq - 4096, (unsigned)DM * NH * DH * 2 + 4096);
    const unsigned ln7 = fresh_lane();
    const unsigned pc_row = (ln7 >> 2) & 7, pc_col = 64 * (ln7 >> 5) + 16 * ((ln7 & 3) ^ ((ln7 >> 4) & 1));
    {
      unsigned vo[4];
#pragma unroll
      for (int m = 0; m < 4; ++m) vo[m] = 4096u + 65536u * (m >> 1) + 128u * (m & 1) + ((pc_row * 8192 + pc_col) ^ (((m >> 1) & 1) ? 32u : 0u)) - 1024u * (m & 3);
      auto w_issue = [&](int s) {
        dma_tile<false>(Lbase + STG + (s % 3) * STG_BYTES + STG_W, vo, srdW, (unsigned)(kslot(s) * 64 * 8192 + hp * 256), 131072u); };
      w_issue(0); w_issue(1);
      {
        unsigned e0;
        asm volatile("s_load_dword %0, %1, 0x0\n\ts_waitcnt lgkmcnt(0)" : "=s"(e0) : "s"(ws + WS_EPOCH) : "memory");
        flag_set(flagv + F_TAG, e0 + 1u);
      }
      for (int s = 0; s < 10; ++s) {
        if (s < 9) WAIT_VM(16); else WAIT_VM(0);
        bar();
        if (s + 2 < 10) w_issue(s + 2);
      }
      bar();
    }
    unsigned vh[4];
#pragma unroll
    for (int m = 0; m < 4; ++m) vh[m] = 4096u + 65536u * m + ((pc_row * 8192 + pc_col) ^ ((m & 1) ? 32u : 0u)) - 1024u * (m & 3);
    int slot = 2;
    auto issue = [&](int n) {
      const int pass = n >= 5 ? 1 : 0, i = n - 5 * pass;
      const unsigned col = (unsigned)((hp + 16) * 256 + 128 * pass), dst = Lbase + WRING + slot * SLOT;
#pragma unroll
      for (int e = 0; e < 2; ++e) {
        const unsigned so = (unsigned)(kslot(2 * i + e) * 64 * 8192) + col;
        dma4<false>(dst + 8192 * e, vh[0], vh[1], vh[2], vh[3], srdW, so);
        dma4<false>(dst + 8192 * e + 4096, vh[0], vh[1], vh[2], vh[3], srdW, so + 4u * 65536u);
      }
      slot = slot + 1 == NWSLOT ? 0 : slot + 1;
    };
    ring_loader<NWSLOT, 10>(L, flagv + F_FULLW, FLAG_OFF + F_FREEW, 0, issue, [&](int) { return 3; },
                            [&](int S) { return S != 2 || rfl(flag_min3(L, FLAG_OFF + F_QREAD)) >= 1u; });
  }

  {
    const int p = hp >> 2, cq = hp & 3;
    const unsigned ln = fresh_lane();
    const unsigned tag = rfl(flag_get(L, FLAG_OFF + F_TAG));
    const u32x4 srdO = make_srd(ws + WS_OT, 96 * 128 * 1024), srdWt = make_srd(ws + WS_WOT, 128 * 40 * 1024);
    auto touch_o = [&](int u) {
#pragma unroll
      for (int i = 0; i < 3; ++i) {
        const unsigned idx = i * 64 + ln, fr = idx >> 3;
        touch64(Lbase + DUMMY, (fr >> 2) * 131072u + (fr & 3) * 1024u + (idx & 7) * 128u, srdO, (unsigned)((6 * b * 128 + 64 * u + 16 * p + 4 * cq) * 1024));
      }
    };
    if (wave == 7) {
      spin_slow<true, true>(L, FLAG_OFF + F_FREE, TOUCH_AT); CBAR();
      poll_tag<128>(ws + WS_FLW, tag, L);
      poll_tag<128>(ws + WS_FLW + 512, tag, L);
      if (blockIdx.x == 0 && ln == 0) __hip_atomic_store((gu32*)(ws + WS_EPOCH), tag, RLX_AGENT);
#pragma unroll
      for (int i = 0; i < 5; ++i) touch64(Lbase + DUMMY, (i * 64 + ln) * 128u, srdWt, (unsigned)((64 * (cc >> 4) + 16 * p + (cc & 15)) * 40 * 1024));
      poll_tag<4>(ws + WS_FLO + (size_t)(4 * b + p) * 128, tag, L);
      touch_o(0);
    }
    bar();
    const int koff = 2 * (b & 7) + 4 * cq;
    if (wave >= 4) {
      const int l = wave - 4;
      const unsigned v0 = ln * 16, vs = v0 + (131072u - 1024u);
      const u32x4 sA = l < 3 ? srdWt : srdO, sB = l < 2 ? srdWt : srdO;
      const unsigned strA = l < 3 ? 40960u : 1024u, strB = l < 2 ? 40960u : 1024u;
      const unsigned basA = (l < 3 ? (unsigned)((10 * cq + 4 * l) * 1024) : (unsigned)((6 * b + 2) * 131072)) + strA * 16u * p;
      const unsigned basB = (l < 2 ? (unsigned)((10 * cq + 4 * l + 2) * 1024) : (unsigned)((6 * b + (l == 2 ? 0 : 4)) * 131072)) + strB * 16u * p;
      const unsigned vA = l < 3 ? v0 : vs, vB = l < 2 ? v0 : vs;
      auto part = [&](int t) {
        const unsigned kk = ((unsigned)(t + koff) & 15u) + (((unsigned)t & 16u) << 2), dst = Lbase + (t & 7) * SLOT + 4096 * l;
        dma2s(dst, v0, vA, sA, basA + strA * kk); dma2s(dst + 2048, v0, vB, sB, basB + strB * kk);
      };
      for (int t = 0; t < 6; ++t) part(t);
#pragma nounroll
      for (int u = 0; u < 16; ++u) {
        if (u < 14) WAIT_VM(16); else WAIT_VM(0);
        bar();
        if (u + 3 < 16) { part(2 * u + 6); part(2 * u + 7); }
        if (u == 4 && wave == 7) {
          poll_tag<4>(ws + WS_FLO + (size_t)(4 * b + p) * 128 + 16, tag, L);
          touch_o(1);
        }
      }
    } else {
      const int qh = wave >> 1, nh = wave & 1;
      f32x4 acc[5][3];
#pragma unroll
      for (int i = 0; i < 5; ++i)
#pragma unroll
        for (int j = 0; j < 3; ++j) acc[i][j] = f32x4{0.f, 0.f, 0.f, 0.f};
      bf16x8 wb[2][2][5], ob[2][2][3];
      LAS char* fw = L + nh * 5120 + ln * 16;
      LAS char* fo = L + 10240 + qh * 3072 + ln * 16;
      auto stp = [&](int pp, int u, bool mf, bool rdd) {
        LAS char* bw = fw + (u & 3) * (2 * SLOT); LAS char* bo = fo + (u & 3) * (2 * SLOT);
#pragma unroll
        for (int e = 0; e < 30; ++e) {
          const int c2 = e / 15, i = (e % 15) / 3, j = e % 3;
          if (mf) acc[i][j] = MFMA16(wb[pp ^ 1][c2][i], ob[pp ^ 1][c2][j], acc[i][j]);
          if (rdd && e < 16) { const int t2 = e >> 3, f = e & 7; if (f < 5) wb[pp][t2][f] = ld_b128(bw + t2 * SLOT + f * 1024); else ob[pp][t2][f - 5] = ld_b128(bo + t2 * SLOT + (f - 5) * 1024); }
          SB();
        }
      };
#pragma unroll
      for (int t2 = 0; t2 < 2; ++t2) {
#pragma unroll
        for (int i = 0; i < 5; ++i) wb[1][t2][i] = bf16x8{};
#pragma unroll
        for (int j = 0; j < 3; ++j) ob[1][t2][j] = bf16x8{};
      }
#pragma nounroll
      for (int u = 0; u < 16; u += 2) { bar(); stp(0, u, true, true); bar(); stp(1, u + 1, true, true); }
      stp(0, 16, true, false);
      const __amdgpu_buffer_rsrc_t rsP = __builtin_amdgcn_make_buffer_rsrc((void*)(ws + WS_PART), 0, 16 * 4 * 60 * 4 * 1024, 0x00020000);
      const unsigned pb = (unsigned)((((b * 4 + cq) * 60 + 30 * qh + 5 * nh) * 4 + p) * 1024) + ln * 16;
#pragma unroll
      for (int i = 0; i < 5; ++i)
#pragma unroll
        for (int j = 0; j < 3; ++j) __builtin_amdgcn_raw_buffer_store_b128(__builtin_bit_cast(u32x4, acc[i][j]), rsP, pb + (unsigned)((j * 10 + i) * 4096), 0, AUX_SC1);
      WAIT_VM(0);
    }
    bar();
    if (wave == 7) {
      if (ln == 0) __hip_atomic_store((gu32*)(ws + WS_FLP + (size_t)(4 * b + cq) * 64 + p * 4), tag, RLX_AGENT);
      poll_tag<4>(ws + WS_FLP + (size_t)(4 * b + cq) * 64, tag, L);
    }
    bar();
    {
      const __amdgpu_buffer_rsrc_t rsP = __builtin_amdgcn_make_buffer_rsrc((void*)(ws + WS_PART), 0, 16 * 4 * 60 * 4 * 1024, 0x00020000);
      u32x4 pv[2][4];
#pragma unroll
      for (int e = 0; e < 2; ++e) {
        const int F = 15 * p + (wave + 8 * e < 15 ? wave + 8 * e : 14);
#pragma unroll
        for (int g2 = 0; g2 < 4; ++g2) pv[e][g2] = __builtin_amdgcn_raw_buffer_load_b128(rsP, (unsigned)((((b * 4 + cq) * 60 + F) * 4 + g2) * 1024) + ln * 16, 0, AUX_SC1);
      }
#pragma unroll
      for (int e = 0; e < 2; ++e) {
        if (wave + 8 * e < 15) {
          const int F = 15 * p + wave + 8 * e, qb = F / 10, nb = F % 10;
          const f32x4 sum = (__builtin_bit_cast(f32x4, pv[e][0]) + __builtin_bit_cast(f32x4, pv[e][1])) + (__builtin_bit_cast(f32x4, pv[e][2]) + __builtin_bit_cast(f32x4, pv[e][3]));
          u32x2 v = {cvtpk(sum[0], sum[1]), cvtpk(sum[2], sum[3])};
          *(u32x2*)(out + (size_t)(96 * b + 16 * qb + (ln & 15)) * DM + 160 * cq + 16 * nb + 4 * (ln >> 4)) = v;
        }
      }
    }
  }
  asm volatile(".Lmha_code_end:");
}

extern "C" void kernel_launch(void* const* d_in, const int* in_sizes, int n_in, void* d_out, int out_size, void* d_ws, size_t ws_size, hipStream_t stream) {
  (void)hipFuncSetAttribute((const void*)attn_kernel, hipFuncAttributeMaxDynamicSharedMemorySize, LDS_A);
  attn_kernel<<<256, 512, LDS_A, stream>>>((const char*)d_in[0], (const char*)d_in[1], (const char*)d_in[2], (const char*)d_in[3], (const unsigned short*)d_in[4], (char*)d_ws, (unsigned short*)d_out);
}
```
